```python
import torch

def _softmax(x, axis=-1):
    return torch.softmax(x, dim=axis)
def _gelu(x):
    return torch.nn.functional.gelu(x, approximate="tanh")
def _silu(x):
    return torch.nn.functional.silu(x)
def _sigmoid(x):
    return torch.sigmoid(x)
def _layernorm(x, axis=-1, eps=1e-5):
    m = x.mean(dim=axis, keepdim=True)
    v = x.var(dim=axis, keepdim=True, unbiased=False)
    return (x - m) / torch.sqrt(v + eps)
def _scatter_add(tgt, idx, src, axis=0):
    return tgt.clone().index_add_(axis, idx, src)
def _segment_sum(src, seg, n):
    out = torch.zeros((n,) + src.shape[1:], dtype=src.dtype)
    return out.index_add_(0, seg, src)
def _pad(x, pw):
    flat = tuple(v for pair in reversed(pw) for v in pair)
    return torch.nn.functional.pad(x, flat)

kernel_name = 'standalone__linear_kernel_rfa__prompt'

def setup_inputs():

    _w = (lambda a: (lambda t: t.to(torch.bfloat16) if t.is_floating_point() else t)(torch.as_tensor(a)))
    B, H, Sq, Sk, D, M = 1, 32, 2048, 2048, 128, 64
    return {
        "q": _w((lambda *s: torch.randn(*s, dtype=torch.bfloat16) * 0.05)(B, H, Sq, D)),
        "k": _w((lambda *s: torch.randn(*s, dtype=torch.bfloat16) * 0.8)(B, H, Sk, D)),
        "v": _w((lambda *s: torch.randn(*s, dtype=torch.bfloat16) * 0.8)(B, H, Sk, D)),
        "omega": _w((lambda *s: torch.randn(*s, dtype=torch.bfloat16) * 0.8)(M, D)),
    }

def reference(q, k, v, omega):
    d = q.shape[-1]
    m = omega.shape[0]
    scale = float(d) ** -0.25
    qs = q * scale
    ks = k * scale
    q_proj = torch.einsum("bhsd,md->bhsm", qs, omega)
    k_proj = torch.einsum("bhsd,md->bhsm", ks, omega)
    q_sq = 0.5 * torch.einsum("bhsd,bhsd->bhs", qs, qs)[..., None]
    k_sq = 0.5 * torch.einsum("bhsd,bhsd->bhs", ks, ks)[..., None]
    inv_sqrt_m = float(m) ** -0.5
    phi_q = torch.exp(q_proj - q_sq) * inv_sqrt_m + 1e-6
    phi_k = torch.exp(k_proj - k_sq) * inv_sqrt_m + 1e-6
    kv = torch.einsum("bhsm,bhsd->bhmd", phi_k, v)
    z  = torch.einsum("bhsm->bhm", phi_k)
    num = torch.einsum("bhqm,bhmd->bhqd", phi_q, kv)
    den = torch.einsum("bhqm,bhm->bhq", phi_q, z)[..., None] + 1e-6
    return num / den
```

```cpp
#include <hip/hip_runtime.h>
#include <hip/hip_bf16.h>
#include <stdint.h>

typedef __attribute__((ext_vector_type(8))) short bf16x8;
typedef __attribute__((ext_vector_type(4))) short s16x4;
typedef __attribute__((ext_vector_type(2))) __bf16 bf2;
typedef __attribute__((ext_vector_type(2))) float f32x2;
typedef __attribute__((ext_vector_type(4))) float f32x4;
typedef __attribute__((ext_vector_type(4))) unsigned u32x4;
typedef __attribute__((ext_vector_type(2))) unsigned u32x2;
typedef __attribute__((ext_vector_type(16))) unsigned u32x16;
#define LAS __attribute__((address_space(3)))
#define GAS __attribute__((address_space(1)))
#define DI __device__ __forceinline__
#define MFMA(a, b, c) \
  __builtin_amdgcn_mfma_f32_16x16x32_bf16(__builtin_bit_cast(bf16x8, a), __builtin_bit_cast(bf16x8, b), (c), 0, 0, 0)
#define MFMA16(a, b0, b1, c) \
  __builtin_amdgcn_mfma_f32_16x16x16bf16_1k(__builtin_bit_cast(s16x4, a), __builtin_bit_cast(s16x4, ((u32x2){b0, b1})), (c), 0, 0, 0)
#define RLX_AGENT __ATOMIC_RELAXED, __HIP_MEMORY_SCOPE_AGENT
typedef GAS unsigned long long gu64;

#define WE0 8
#define WE1 8
#define WE2 4
#define WL0 3
#define WL1 8
#define WL2 8
#define SPLITK 12
static_assert(WE0 >= 2 && WE0 < SPLITK && WL0 >= 2 && WL0 < SPLITK && SPLITK <= 16, "the loader joins the barrier at group W0");

constexpr int NH = 32, SEQ = 2048, DH = 128, NM = 64;
constexpr int NCH = 8;
constexpr int ROWS = SEQ / NCH;
constexpr int NWAVE = 10;
constexpr unsigned TENSOR_BYTES = NH * SEQ * DH * 2;
constexpr int SC1 = 16;
constexpr int WT_NT = 19;
constexpr unsigned V_OFF = 0, KQ_OFF = 65536, F_OFF = 126976, P_OFF = 131072, LDS_BYTES = 163840;
constexpr unsigned KVF_OFF = V_OFF, KVF_T = 2048 + 16;
constexpr unsigned K15_OFF = P_OFF + 7 * 4096;
constexpr unsigned Q15_OFF = V_OFF + 20480;
constexpr unsigned F_PROG = F_OFF, F_PHI = F_OFF + 4, F_GRDY = F_OFF + 8, F_KVF = F_OFF + 12, F_HOLD = F_OFF + 16,
                   F_OM = F_OFF + 20, F_K = F_OFF + 24, F_KV4 = F_OFF + 28, F_PUB = F_OFF + 32, F_ZC = F_OFF + 36,
                   F_Z = F_OFF + 256, F_ZP = F_OFF + 1024;
constexpr unsigned WS_EPOCH = 0;
constexpr unsigned WS_FLAG = 4096;
constexpr unsigned WS_Z = 65536;
constexpr unsigned WS_PART = 131072;
constexpr unsigned WS_BYTES = WS_PART + 256 * 16384;

constexpr float SCALE = 0.29730177875068026f;
constexpr float C1 = SCALE * 1.4426950408889634f;

DI unsigned pk2(float a, float b) {
  f32x2 f = {a, b};
  return __builtin_bit_cast(unsigned, __builtin_convertvector(f, bf2));
}
DI float dot2(unsigned a, unsigned b, float c) {
  return __builtin_amdgcn_fdot2_f32_bf16(__builtin_bit_cast(bf2, a), __builtin_bit_cast(bf2, b), c, false);
}
DI float bf_lo(unsigned u) { return __uint_as_float(u << 16); }
DI float xrow_sum(float v) {
  unsigned u = __float_as_uint(v);
  auto a = __builtin_amdgcn_permlane16_swap(u, u, false, false);
  u = __float_as_uint(__uint_as_float(a[0]) + __uint_as_float(a[1]));
  auto b = __builtin_amdgcn_permlane32_swap(u, u, false, false);
  return __uint_as_float(b[0]) + __uint_as_float(b[1]);
}

DI unsigned lds_peek(unsigned addr) {
  unsigned x;
  asm volatile("ds_read_b32 %0, %1\n\ts_waitcnt lgkmcnt(0)" : "=v"(x) : "v"(addr) : "memory");
  return __builtin_amdgcn_readfirstlane(x);
}
DI void lds_wait_ge(unsigned addr, unsigned val) {
  while (lds_peek(addr) < val) {}
}
DI void lds_add(unsigned addr, unsigned val) {
  asm volatile("s_waitcnt lgkmcnt(0)\n\ts_mov_b64 exec, 1\n\tds_add_u32 %0, %1\n\ts_mov_b64 exec, -1" ::"v"(addr), "v"(val) : "memory");
}
DI void lds_or(unsigned addr, unsigned val) {
  asm volatile("s_waitcnt lgkmcnt(0)\n\ts_mov_b64 exec, 1\n\tds_or_b32 %0, %1\n\ts_mov_b64 exec, -1" ::"v"(addr), "v"(val) : "memory");
}
DI void lds_set(unsigned addr, unsigned val) {
  asm volatile("s_waitcnt lgkmcnt(0)\n\ts_mov_b64 exec, 1\n\tds_write_b32 %0, %1\n\ts_mov_b64 exec, -1" ::"v"(addr), "v"(val) : "memory");
}

DI void poll_flags(const unsigned char* line, unsigned lane, unsigned long long tag) {
  const unsigned lo = __builtin_amdgcn_readfirstlane((unsigned)tag), hi = __builtin_amdgcn_readfirstlane((unsigned)(tag >> 32));
  const unsigned long long la = ((unsigned long long)(unsigned)__builtin_amdgcn_readfirstlane((unsigned)((size_t)line >> 32)) << 32) |
                                (unsigned long long)(unsigned)__builtin_amdgcn_readfirstlane((unsigned)(size_t)line);
#pragma unroll 1
  for (unsigned spins = 0; spins < (1u << 20); ++spins) {
    u32x16 x;
    asm volatile("s_nop 4\n\ts_load_dwordx16 %0, %1, 0x0 glc\n\ts_waitcnt lgkmcnt(0)" : "=s"(x) : "s"(la) : "memory");
    unsigned d = 0;
#pragma unroll
    for (int i = 0; i < 8; ++i) d |= (x[2 * i] ^ lo) | (x[2 * i + 1] ^ hi);
    if (d == 0) break;
    if ((spins & 7) == 7 && __all(__hip_atomic_load((gu64*)(line + (lane & 7) * 8), RLX_AGENT) == tag)) break;
    __builtin_amdgcn_s_sleep(1);
  }
  asm volatile("" ::: "memory");
}

#define DMA4(POL, m0v, srd, soff, a0, a1, a2, a3)                                   \
  asm volatile("s_mov_b32 m0, %0\n\ts_nop 0\n\t"                                    \
               "buffer_load_dwordx4 %1, %5, %6 offen " POL " lds\n\t"               \
               "buffer_load_dwordx4 %2, %5, %6 offen offset:1024 " POL " lds\n\t"   \
               "buffer_load_dwordx4 %3, %5, %6 offen offset:2048 " POL " lds\n\t"   \
               "buffer_load_dwordx4 %4, %5, %6 offen offset:3072 " POL " lds"       \
               ::"s"(m0v), "v"(a0), "v"(a1), "v"(a2), "v"(a3), "s"(srd), "s"(soff)  \
               : "m0", "memory")
#define WAITV(n) asm volatile("s_waitcnt vmcnt(%0)" ::"i"(n) : "memory")
#define BARRIER() asm volatile("s_waitcnt lgkmcnt(0)\n\ts_barrier" ::: "memory")

DI float sq_share(const u32x4 (&f)[4]) {
  float s0 = 0.f, s1 = 0.f;
#pragma unroll
  for (int s = 0; s < 4; ++s) {
    s0 = dot2(f[s][0], f[s][0], s0); s1 = dot2(f[s][1], f[s][1], s1);
    s0 = dot2(f[s][2], f[s][2], s0); s1 = dot2(f[s][3], f[s][3], s1);
  }
  asm volatile("" : "+v"(s0), "+v"(s1));
  __builtin_amdgcn_sched_barrier(0);
  return s0 + s1;
}

DI void phi_q(LAS unsigned char* lds, const u32x4 (&om)[4][4], unsigned slot, unsigned rbase, unsigned lane) {
  const unsigned qb = slot + rbase;
  u32x4 qf[4];
#pragma unroll
  for (int s = 0; s < 4; ++s) qf[s] = *(const LAS u32x4*)(lds + (qb ^ (64 * s)));
  const float sq = sq_share(qf);
  f32x4 acc[4];
#pragma unroll
  for (int mt = 0; mt < 4; ++mt) {
    acc[mt] = (f32x4){0.f, 0.f, 0.f, 0.f};
#pragma unroll
    for (int s = 0; s < 4; ++s) acc[mt] = MFMA(om[mt][s], qf[s], acc[mt]);
  }
  const float bias = xrow_sum(sq) * (-0.5f * SCALE * C1) - 3.0f;
#pragma unroll
  for (int mp = 0; mp < 2; ++mp) {
    u32x4 pq;
#pragma unroll
    for (int a = 0; a < 2; ++a)
#pragma unroll
      for (int j = 0; j < 2; ++j)
        pq[2 * a + j] = pk2(__builtin_amdgcn_exp2f(__builtin_fmaf(acc[2 * mp + a][2 * j], C1, bias)) + 1e-6f,
                            __builtin_amdgcn_exp2f(__builtin_fmaf(acc[2 * mp + a][2 * j + 1], C1, bias)) + 1e-6f);
    *(LAS u32x4*)(lds + slot + mp * 1024 + lane * 16) = pq;
  }
}

DI void out_rows(const u32x4 (&kb)[9][2], const u32x4 (&pq)[2], __amdgpu_buffer_rsrc_t srdY, unsigned ob) {
  f32x4 o[9];
#pragma unroll
  for (int t = 8; t >= 0; --t) {
    o[t] = MFMA(pq[0], kb[t][0], ((f32x4){0.f, 0.f, 0.f, 0.f}));
    o[t] = MFMA(pq[1], kb[t][1], o[t]);
  }
#pragma unroll
  for (int i = 0; i < 4; ++i) {
    const float other = __int_as_float(__builtin_amdgcn_update_dpp(0, __float_as_int(o[8][i]), 0xB1, 0xf, 0xf, true));
    const float inv = __builtin_amdgcn_rcpf((o[8][i] + other) + 1e-6f);
    const u32x4 p = {pk2(o[0][i] * inv, o[1][i] * inv), pk2(o[2][i] * inv, o[3][i] * inv),
                     pk2(o[4][i] * inv, o[5][i] * inv), pk2(o[6][i] * inv, o[7][i] * inv)};
    __builtin_amdgcn_raw_buffer_store_b128(p, srdY, ob + i * 256, 0, WT_NT);
  }
}

DI void wait_groups(unsigned left) {
  switch (left) {
    case 0: WAITV(0); break;   case 1: WAITV(4); break;   case 2: WAITV(8); break;   case 3: WAITV(12); break;
    case 4: WAITV(16); break;  case 5: WAITV(20); break;  case 6: WAITV(24); break;  case 7: WAITV(28); break;
    case 8: WAITV(32); break;  default: WAITV(36); break;
  }
}

__global__ void __launch_bounds__(NWAVE * 64, 3)
rfa_kernel(const void* __restrict__ qp, const void* __restrict__ kp, const void* __restrict__ vp,
           const void* __restrict__ op, void* __restrict__ outp, unsigned char* __restrict__ ws) {
  __shared__ __attribute__((aligned(1024))) unsigned char lds_[LDS_BYTES];
  LAS unsigned char* const lds = (LAS unsigned char*)lds_;
  const unsigned lane = threadIdx.x & 63;
  const unsigned wv = __builtin_amdgcn_readfirstlane(threadIdx.x >> 6);
  const unsigned b = blockIdx.x;
  const unsigned head = (b & 7) * 4 + (b >> 6), c = (b >> 3) & 7;
  const unsigned row0_b = (head * SEQ + c * ROWS) * 256;
  const unsigned me = head * NCH + c;
  const unsigned n = lane & 15, g = lane >> 4;
  const unsigned rot = (b >> 3) & 15;
#define GOFF(i) (row0_b + ((((i) + rot) & 15u) << 12))

  if (__builtin_expect(wv == 0, 1)) {
    __builtin_amdgcn_s_setprio(3);
    const __amdgpu_buffer_rsrc_t srdQ = __builtin_amdgcn_make_buffer_rsrc((void*)qp, 0, TENSOR_BYTES, 0x00020000);
    const __amdgpu_buffer_rsrc_t srdK = __builtin_amdgcn_make_buffer_rsrc((void*)kp, 0, TENSOR_BYTES, 0x00020000);
    const __amdgpu_buffer_rsrc_t srdV = __builtin_amdgcn_make_buffer_rsrc((void*)vp, 0, TENSOR_BYTES, 0x00020000);
    const __amdgpu_buffer_rsrc_t srdO = __builtin_amdgcn_make_buffer_rsrc((void*)op, 0, NM * DH * 2, 0x00020000);
    const unsigned r0 = g * 256 + 16 * (n ^ g), r1 = r0 ^ 64, r2 = r0 ^ 128, r3 = r0 ^ 192;
    const unsigned t0 = g * 256 + 16 * (n ^ (g << 1)), t1 = t0 ^ 128;
#pragma unroll
    for (int i = 0; i < 4; ++i) DMA4("", P_OFF + i * 4096u, srdO, i * 4096u, r0, r1, r2, r3);
    DMA4("nt", KQ_OFF, srdK, GOFF(0), r0, r1, r2, r3);
    DMA4("nt", KQ_OFF + 4096u, srdK, GOFF(1), r0, r1, r2, r3);
    if (lane < 16) *(volatile LAS unsigned*)(lds + F_OFF + lane * 4) = 0u;
    int issued = 6, landed = 0;
#define LANDED(W) do { WAITV(4 * (W)); if (issued - (W) > landed) { landed = issued - (W); lds_set(F_PROG, landed); } } while (0)
#define STEP(k, lo, hi) if ((k) >= (lo) && (k) < (hi)) LANDED(k);
#define LADDER(lo, hi) STEP(9, lo, hi) STEP(8, lo, hi) STEP(7, lo, hi) STEP(6, lo, hi) STEP(5, lo, hi) STEP(4, lo, hi) \
    STEP(3, lo, hi) STEP(2, lo, hi) STEP(1, lo, hi) STEP(0, lo, hi)
#define KGROUP(i) DMA4("nt", (i) == 15 ? K15_OFF : KQ_OFF + (i) * 4096u, srdK, GOFF(i), r0, r1, r2, r3)
#define VGROUP(i) DMA4("nt", V_OFF + (i) * 4096u, srdV, GOFF(((i) >> 1) + ((i) & 1) * 8), t0, t1, t0, t1)
#define STREAM(W0, W1, W2)                                                                             \
    LADDER(W0, 6)                                                                                      \
    _Pragma("unroll 1") for (unsigned i = 2; i < SPLITK; ++i) {                                        \
      KGROUP(i); ++issued; LANDED(W0);                                                                 \
      if (i == (W0)) BARRIER();                                                                        \
    }                                                                                                  \
    _Pragma("unroll 1") for (unsigned i = SPLITK; i < 16; ++i) { KGROUP(i); ++issued; LANDED(W1); }    \
    _Pragma("unroll 1") for (unsigned i = 0; i < 16; ++i) { VGROUP(i); ++issued; LANDED(W1); }         \
    LADDER(W2, W1)                                                                                     \
    lds_wait_ge(F_K, 8);                                                                               \
    _Pragma("unroll 1") for (unsigned i = 0; i < 16; ++i) {                                            \
      if (i == 15) lds_wait_ge(F_KV4, 8);                                                              \
      DMA4("nt", i == 15 ? Q15_OFF : KQ_OFF + i * 4096u, srdQ, GOFF(i), r0, r1, r2, r3);               \
      ++issued;                                                                                        \
      unsigned hold;                                                                                   \
      asm volatile("ds_read_b32 %0, %1\n\ts_waitcnt vmcnt(%2) lgkmcnt(0)" : "=v"(hold) : "v"(F_HOLD), "i"(4 * (W2)) : "memory"); \
      if (issued - (W2) > landed) { landed = issued - (W2); lds_set(F_PROG, landed); }                 \
      if (__builtin_amdgcn_readfirstlane(hold)) {                                                      \
        do {                                                                                           \
          if (issued != landed) { wait_groups(issued - landed - 1); lds_set(F_PROG, ++landed); }       \
        } while (lds_peek(F_HOLD));                                                                    \
      }                                                                                                \
    }                                                                                                  \
    LADDER(0, W2)
    if ((b >> 6) < 2) { STREAM(WE0, WE1, WE2) } else { STREAM(WL0, WL1, WL2) }
    return;
  }

  const __amdgpu_buffer_rsrc_t srdW = __builtin_amdgcn_make_buffer_rsrc(ws, 0, WS_BYTES, 0x00020000);

  if (wv == 1) {
    __builtin_amdgcn_s_setprio(2);
    const unsigned long long ep = __hip_atomic_load((gu64*)(ws + WS_EPOCH + head * 128), RLX_AGENT);
    BARRIER();
    const unsigned long long tag = ep + 1ull;
    lds_wait_ge(F_ZC, 8);
    float zp[8];
#pragma unroll
    for (int i = 0; i < 8; ++i) zp[i] = *(const LAS float*)(lds + F_ZP + i * 256 + lane * 4);
    const float zme = ((zp[0] + zp[1]) + (zp[2] + zp[3])) + ((zp[4] + zp[5]) + (zp[6] + zp[7]));
    __builtin_amdgcn_raw_buffer_store_b32(__float_as_uint(zme), srdW, WS_Z + me * 256 + lane * 4, 0, SC1);
    WAITV(0);
    lds_wait_ge(F_PUB, 8);
    if (lane == 0) __hip_atomic_store((gu64*)(ws + WS_FLAG + head * 128 + c * 8), tag, RLX_AGENT);
    poll_flags(ws + WS_FLAG + head * 128, lane, tag);
    lds_set(F_HOLD, 8);
    lds_set(F_GRDY, 1);
#pragma unroll
    for (int i = 0; i < 8; ++i)
      zp[i] = __uint_as_float(__builtin_amdgcn_raw_buffer_load_b32(srdW, WS_Z + lane * 4, (head * NCH + i) * 256, SC1));
    const float zs = ((zp[0] + zp[1]) + (zp[2] + zp[3])) + ((zp[4] + zp[5]) + (zp[6] + zp[7]));
    const unsigned zh = pk2(zs, 0.f), zl = pk2(zs - bf_lo(zh), 0.f);
    const unsigned zo = F_Z + (lane >> 5) * 128 + ((lane >> 2) & 3) * 16 + ((lane >> 4) & 1) * 8 + (lane & 3) * 2;
    *(LAS unsigned short*)(lds + zo) = (unsigned short)zh;
    *(LAS unsigned short*)(lds + zo + 64) = (unsigned short)zl;
    lds_add(F_KVF, 1);
    if (c == 0 && lane == 0) __hip_atomic_store((gu64*)(ws + WS_EPOCH + head * 128), tag, RLX_AGENT);
    return;
  }

  const unsigned w = wv - 2;
  const unsigned pofs = WS_PART + w * 2048 + lane * 16;
  const unsigned rbase = n * 256 + 16 * (g ^ n);
  BARRIER();
  lds_wait_ge(F_PROG, 4);
  u32x4 om[4][4];
#pragma unroll
  for (int mt = 0; mt < 4; ++mt)
#pragma unroll
    for (int s = 0; s < 4; ++s) om[mt][s] = *(const LAS u32x4*)(lds + P_OFF + mt * 4096 + (rbase ^ (64 * s)));
  lds_add(F_OM, 1);

  {
    u32x4 p[4];
    const u32x4 ones3 = {0x3f803f80u, 0x00003f80u, 0u, 0u};
#pragma unroll
    for (int kt = 0; kt < 2; ++kt) {
      lds_wait_ge(F_PROG, 5 + w + 8 * kt);
      const unsigned kb = rbase + ((kt && w == 7) ? K15_OFF : KQ_OFF + (w + 8 * kt) * 4096);
      u32x4 kf[4];
#pragma unroll
      for (int s = 0; s < 4; ++s) kf[s] = *(const LAS u32x4*)(lds + (kb ^ (64 * s)));
      if (kt) lds_add(F_K, 1);
      const float sq = sq_share(kf);
      f32x4 acc[4];
#pragma unroll
      for (int mt = 0; mt < 4; ++mt) {
        acc[mt] = (f32x4){0.f, 0.f, 0.f, 0.f};
#pragma unroll
        for (int s = 0; s < 4; ++s) acc[mt] = MFMA(kf[s], om[mt][s], acc[mt]);
      }
      const float t = sq * (-0.5f * SCALE * C1);
      const unsigned p0 = pk2(t, 0.f);
      const float t1 = t - bf_lo(p0);
      const unsigned p1 = pk2(t1, 0.f);
      const float t2 = t1 - bf_lo(p1);
      const u32x4 au = {(p0 & 0xffffu) | (p1 << 16), pk2(t2, 0.f) & 0xffffu, 0u, 0u};
      const f32x4 sb = MFMA(au, ones3, ((f32x4){-3.f, -3.f, -3.f, -3.f}));
#pragma unroll
      for (int mt = 0; mt < 4; ++mt)
#pragma unroll
        for (int j = 0; j < 2; ++j) {
          const float e0 = __builtin_amdgcn_exp2f(__builtin_fmaf(acc[mt][2 * j], C1, sb[2 * j])) + 1e-6f;
          const float e1 = __builtin_amdgcn_exp2f(__builtin_fmaf(acc[mt][2 * j + 1], C1, sb[2 * j + 1])) + 1e-6f;
          p[mt][2 * kt + j] = pk2(e0, e1);
        }
      if (kt == 0) {
#pragma unroll
        for (int mt = 0; mt < 4; ++mt) asm volatile("" : "+v"(p[mt][0]), "+v"(p[mt][1]));
        lds_wait_ge(F_OM, 8);
      }
    }
#pragma unroll
    for (int mt = 0; mt < 4; ++mt) *(LAS u32x4*)(lds + P_OFF + w * 4096 + mt * 1024 + lane * 16) = p[mt];
    lds_or(F_PHI, 1u << w);
    const u32x4 ones = {0x3f803f80u, 0x3f803f80u, 0x3f803f80u, 0x3f803f80u};
    f32x4 zacc[4];
#pragma unroll
    for (int mt = 0; mt < 4; ++mt) zacc[mt] = MFMA(p[mt], ones, ((f32x4){0.f, 0.f, 0.f, 0.f}));
    if (n == 0) {
#pragma unroll
      for (int mt = 0; mt < 4; ++mt) *(LAS f32x4*)(lds + F_ZP + w * 256 + mt * 64 + g * 16) = zacc[mt];
    }
    lds_add(F_ZC, 1);
  }

  const unsigned mp_w = w & 1, dj_w = w >> 1;
  f32x4 kv[2][2];
#pragma unroll
  for (int i = 0; i < 4; ++i) kv[i >> 1][i & 1] = (f32x4){0.f, 0.f, 0.f, 0.f};
  {
    const unsigned qq = n >> 2, p = lane & 3;
    const unsigned ta0 = V_OFF + 256 * (4 * g + qq) + 16 * ((4 * dj_w + (p >> 1)) ^ ((4 * (g & 1) + qq) << 1)) + 8 * (p & 1);
    const unsigned ta1 = ta0 ^ 32;
    const unsigned pa = P_OFF + mp_w * 2048 + lane * 16;
    unsigned seen = 0, phim = 0;
    u32x4 a0, a1;
    bf16x8 b0, b1;
#pragma unroll
    for (int j = 0; j <= 8; ++j) {
      u32x4 na0, na1;
      bf16x8 nb0, nb1;
      if (j < 8) {
        while (seen < 22u + 2 * j || !((phim >> j) & 1)) {
          u32x2 x;
          asm volatile("ds_read_b64 %0, %1\n\ts_waitcnt lgkmcnt(0)" : "=v"(x) : "v"(F_PROG) : "memory");
          seen = __builtin_amdgcn_readfirstlane(x[0]);
          phim = __builtin_amdgcn_readfirstlane(x[1]);
        }
        asm volatile("" ::: "memory");
        const s16x4 b00 = __builtin_amdgcn_ds_read_tr16_b64_v4i16((LAS s16x4*)(lds + ta0 + j * 8192));
        const s16x4 b01 = __builtin_amdgcn_ds_read_tr16_b64_v4i16((LAS s16x4*)(lds + ta0 + j * 8192 + 4096));
        na0 = *(const LAS u32x4*)(lds + pa + j * 4096);
        na1 = *(const LAS u32x4*)(lds + pa + j * 4096 + 1024);
        const s16x4 b10 = __builtin_amdgcn_ds_read_tr16_b64_v4i16((LAS s16x4*)(lds + ta1 + j * 8192));
        const s16x4 b11 = __builtin_amdgcn_ds_read_tr16_b64_v4i16((LAS s16x4*)(lds + ta1 + j * 8192 + 4096));
        nb0 = __builtin_shufflevector(b00, b01, 0, 1, 2, 3, 4, 5, 6, 7);
        nb1 = __builtin_shufflevector(b10, b11, 0, 1, 2, 3, 4, 5, 6, 7);
        if (j == 4) lds_add(F_KV4, 1);
        __builtin_amdgcn_sched_barrier(0);
      }
      if (j > 0) {
        kv[0][0] = MFMA(a0, b0, kv[0][0]);
        kv[1][0] = MFMA(a1, b0, kv[1][0]);
        kv[0][1] = MFMA(a0, b1, kv[0][1]);
        kv[1][1] = MFMA(a1, b1, kv[1][1]);
        __builtin_amdgcn_sched_barrier(0);
      }
      if (j < 8) { a0 = na0; a1 = na1; b0 = nb0; b1 = nb1; }
    }
  }
#pragma unroll
  for (int ct = 0; ct < 2; ++ct) {
    const u32x4 p = {pk2(kv[0][ct][0], kv[0][ct][1]), pk2(kv[0][ct][2], kv[0][ct][3]),
                     pk2(kv[1][ct][0], kv[1][ct][1]), pk2(kv[1][ct][2], kv[1][ct][3])};
    __builtin_amdgcn_raw_buffer_store_b128(p, srdW, pofs + me * 16384 + ct * 1024, 0, SC1);
  }
  WAITV(0);
  lds_add(F_PUB, 1);

  const unsigned slot_a = KQ_OFF + w * 4096, slot_b = w == 7 ? Q15_OFF : KQ_OFF + 32768 + w * 4096;
  unsigned qi = 0;
  for (;;) {
    u32x4 f;
    asm volatile("ds_read_b128 %0, %1\n\ts_waitcnt lgkmcnt(0)" : "=v"(f) : "v"(F_OFF) : "memory");
    if (__builtin_amdgcn_readfirstlane(f[2])) break;
    if (qi < 2 && __builtin_amdgcn_readfirstlane(f[0]) >= 37 + w + 8 * qi) {
      phi_q(lds, om, qi ? slot_b : slot_a, rbase, lane);
      ++qi;
    }
  }
  {
    u32x4 px[7][2];
#pragma unroll
    for (int i = 0; i < 7; ++i) {
      const unsigned o = head * NCH + ((c + 1 + i) & 7);
      px[i][0] = __builtin_amdgcn_raw_buffer_load_b128(srdW, pofs, o * 16384, SC1);
      px[i][1] = __builtin_amdgcn_raw_buffer_load_b128(srdW, pofs + 1024, o * 16384, SC1);
      __builtin_amdgcn_sched_barrier(0);
    }
    lds_add(F_HOLD, 0xffffffffu);
    __builtin_amdgcn_sched_barrier(0);
    const unsigned one = (n >> 2) == g ? ((n & 1) ? 0x3f800000u : 0x00003f80u) : 0u;
    const u32x2 eye = {(n & 2) ? 0u : one, (n & 2) ? one : 0u};
#pragma unroll
    for (int i = 0; i < 7; ++i) {
#pragma unroll
      for (int ct = 0; ct < 2; ++ct) {
        kv[0][ct] = MFMA16(eye, px[i][ct][0], px[i][ct][1], kv[0][ct]);
        kv[1][ct] = MFMA16(eye, px[i][ct][2], px[i][ct][3], kv[1][ct]);
      }
      __builtin_amdgcn_sched_barrier(0);
    }
    const unsigned ko = KVF_OFF + (n & 7) * KVF_T + mp_w * 1024 + (4 * dj_w + (n >> 3) + 16 * g) * 16;
#pragma unroll
    for (int ct = 0; ct < 2; ++ct) {
      const u32x4 p = {pk2(kv[0][ct][0], kv[0][ct][1]), pk2(kv[0][ct][2], kv[0][ct][3]),
                       pk2(kv[1][ct][0], kv[1][ct][1]), pk2(kv[1][ct][2], kv[1][ct][3])};
      *(LAS u32x4*)(lds + ko + ct * 32) = p;
    }
    lds_add(F_KVF, 1);
  }
  while (qi < 2) {
    lds_wait_ge(F_PROG, 37 + w + 8 * qi);
    phi_q(lds, om, qi ? slot_b : slot_a, rbase, lane);
    ++qi;
  }
  if (w < 4) __builtin_amdgcn_s_setprio(1);
  {
    const __amdgpu_buffer_rsrc_t srdY = __builtin_amdgcn_make_buffer_rsrc(outp, 0, TENSOR_BYTES, 0x00020000);
    u32x4 kb[9][2], pa[2], pb[2];
    pa[0] = *(const LAS u32x4*)(lds + slot_a + lane * 16); pa[1] = *(const LAS u32x4*)(lds + slot_a + 1024 + lane * 16);
    pb[0] = *(const LAS u32x4*)(lds + slot_b + lane * 16); pb[1] = *(const LAS u32x4*)(lds + slot_b + 1024 + lane * 16);
    lds_wait_ge(F_KVF, 9);
    kb[8][0] = *(const LAS u32x4*)(lds + F_Z + (lane & 1) * 64 + g * 16);
    kb[8][1] = *(const LAS u32x4*)(lds + F_Z + 128 + (lane & 1) * 64 + g * 16);
#pragma unroll
    for (int t = 7; t >= 0; --t) {
      kb[t][0] = *(const LAS u32x4*)(lds + KVF_OFF + t * KVF_T + lane * 16);
      kb[t][1] = *(const LAS u32x4*)(lds + KVF_OFF + t * KVF_T + 1024 + lane * 16);
    }
    __builtin_amdgcn_sched_barrier(0);
    out_rows(kb, pa, srdY, GOFF(w) + g * 1024 + n * 16);
    __builtin_amdgcn_sched_barrier(0);
    out_rows(kb, pb, srdY, GOFF(w + 8) + g * 1024 + n * 16);
  }
}

extern "C" void kernel_launch(void* const* d_in, const int* in_sizes, int n_in, void* d_out, int out_size,
                              void* d_ws, size_t ws_size, hipStream_t stream) {
  (void)in_sizes; (void)n_in; (void)out_size; (void)ws_size;
  rfa_kernel<<<dim3(256), dim3(NWAVE * 64), 0, stream>>>(d_in[0], d_in[1], d_in[2], d_in[3], d_out, (unsigned char*)d_ws);
}
```
